```python
import torch

def _softmax(x, axis=-1):
    return torch.softmax(x, dim=axis)
def _gelu(x):
    return torch.nn.functional.gelu(x, approximate="tanh")
def _silu(x):
    return torch.nn.functional.silu(x)
def _sigmoid(x):
    return torch.sigmoid(x)
def _layernorm(x, axis=-1, eps=1e-5):
    m = x.mean(dim=axis, keepdim=True)
    v = x.var(dim=axis, keepdim=True, unbiased=False)
    return (x - m) / torch.sqrt(v + eps)
def _scatter_add(tgt, idx, src, axis=0):
    return tgt.clone().index_add_(axis, idx, src)
def _segment_sum(src, seg, n):
    out = torch.zeros((n,) + src.shape[1:], dtype=src.dtype)
    return out.index_add_(0, seg, src)
def _pad(x, pw):
    flat = tuple(v for pair in reversed(pw) for v in pair)
    return torch.nn.functional.pad(x, flat)

kernel_name = 'standalone__grn__sampling'

def setup_inputs():

    _w = (lambda a: (lambda t: t.to(torch.bfloat16) if t.is_floating_point() else t)(torch.as_tensor(a)))
    B, S, D = 1024, 8, 1024
    return {
        "x": _w((lambda *s: torch.randn(*s, dtype=torch.bfloat16) * 0.05)(B, S, D)),
        "g": _w((lambda *s: torch.randn(*s, dtype=torch.bfloat16) * 0.4)(D) * 0.1),
        "b": _w((lambda *s: torch.randn(*s, dtype=torch.bfloat16) * 0.1)(D) * 0.1),
    }

def reference(x, g, b):
    gx = torch.sqrt((x * x).sum(axis=-2, keepdims=True))
    nx = gx / (gx.mean(axis=-1, keepdims=True) + 1e-6)
    return g * (x * nx) + b + x
```

```cpp
#include <hip/hip_runtime.h>
#include <hip/hip_bf16.h>

namespace {

typedef unsigned int u32;
typedef u32 u32x4 __attribute__((ext_vector_type(4)));
typedef float f32x2 __attribute__((ext_vector_type(2)));
typedef __bf16 bf16x2 __attribute__((ext_vector_type(2)));

constexpr int S = 8;
constexpr int D = 1024;
constexpr int ROW_BYTES = D * 2;
constexpr int THREADS = D / 8;

__device__ __forceinline__ f32x2 unpack_bf16x2(u32 v) {
    return f32x2{__uint_as_float(v << 16), __uint_as_float(v & 0xffff0000u)};
}
__device__ __forceinline__ u32 pack_bf16x2(f32x2 v) {
    return __builtin_bit_cast(u32, __builtin_convertvector(v, bf16x2));
}
template <int CTRL, int ROW_MASK>
__device__ __forceinline__ float dpp_add(float v) {
    return v + __int_as_float(__builtin_amdgcn_update_dpp(0, __float_as_int(v), CTRL, ROW_MASK, 0xf, true));
}
__device__ __forceinline__ float wave_sum_lane63(float v) {
    v = dpp_add<0xB1, 0xf>(v);
    v = dpp_add<0x4E, 0xf>(v);
    v = dpp_add<0x141, 0xf>(v);
    v = dpp_add<0x140, 0xf>(v);
    v = dpp_add<0x142, 0xa>(v);
    v = dpp_add<0x143, 0xc>(v);
    return v;
}

__global__ __launch_bounds__(THREADS)
void grn_kernel(const char* __restrict__ x, const char* __restrict__ g,
                const char* __restrict__ b, char* __restrict__ out) {
    __shared__ float red[THREADS];

    const u32 tid = threadIdx.x;
    const u32 col = tid * 16;
    u32 o0 = blockIdx.x * (S * ROW_BYTES) + col + 2 * ROW_BYTES;
    u32 o1 = o0 + 4 * ROW_BYTES;
    asm volatile("" : "+v"(o0), "+v"(o1));

    u32x4 xv[S];
#pragma unroll
    for (int r = 0; r < S; ++r)
        xv[r] = __builtin_nontemporal_load(
            (const u32x4*)(x + (size_t)(r < 4 ? o0 : o1) + ((r & 3) - 2) * ROW_BYTES));
    const u32x4 gv = *(const u32x4*)(g + (size_t)col);
    const u32x4 bv = *(const u32x4*)(b + (size_t)col);
    __builtin_amdgcn_sched_barrier(0);

    f32x2 xf[S][4], ss[4];
#pragma unroll
    for (int r = 0; r < S; ++r) {
#pragma unroll
        for (int j = 0; j < 4; ++j) {
            const f32x2 t = unpack_bf16x2(xv[r][j]);
            xf[r][j] = t;
            ss[j] = r == 0 ? t * t : __builtin_elementwise_fma(t, t, ss[j]);
        }
        asm volatile("" : "+v"(ss[0]), "+v"(ss[1]), "+v"(ss[2]), "+v"(ss[3]));
        __builtin_amdgcn_sched_barrier(0);
    }

    f32x2 gx[4];
#pragma unroll
    for (int j = 0; j < 4; ++j)
        gx[j] = f32x2{__builtin_amdgcn_sqrtf(ss[j].x), __builtin_amdgcn_sqrtf(ss[j].y)};
    const f32x2 p = (gx[0] + gx[1]) + (gx[2] + gx[3]);
    red[tid] = wave_sum_lane63(p.x + p.y);

    f32x2 ggx[4], bf[4];
#pragma unroll
    for (int j = 0; j < 4; ++j) {
        ggx[j] = unpack_bf16x2(gv[j]) * gx[j];
        bf[j] = unpack_bf16x2(bv[j]);
        asm volatile("" : "+v"(ggx[j]), "+v"(bf[j]));
    }
    __syncthreads();

    const float inv = __builtin_amdgcn_rcpf((red[63] + red[127]) * (1.0f / D) + 1e-6f);
    f32x2 sc[4];
#pragma unroll
    for (int j = 0; j < 4; ++j)
        sc[j] = __builtin_elementwise_fma(ggx[j], f32x2{inv, inv}, f32x2{1.0f, 1.0f});

#pragma unroll
    for (int r = 0; r < S; ++r) {
        u32x4 o;
#pragma unroll
        for (int j = 0; j < 4; ++j)
            o[j] = pack_bf16x2(__builtin_elementwise_fma(xf[r][j], sc[j], bf[j]));
        __builtin_nontemporal_store(
            o, (u32x4*)(out + (size_t)(r < 4 ? o0 : o1) + ((r & 3) - 2) * ROW_BYTES));
    }
}

}

extern "C" void kernel_launch(void* const* d_in, const int* in_sizes, int n_in,
                              void* d_out, int out_size, void* d_ws, size_t ws_size,
                              hipStream_t stream) {
    const int batches = in_sizes[0] / (S * D);
    grn_kernel<<<batches, THREADS, 0, stream>>>((const char*)d_in[0], (const char*)d_in[1],
                                                (const char*)d_in[2], (char*)d_out);
}
```
